```python
import torch

def _softmax(x, axis=-1):
    return torch.softmax(x, dim=axis)
def _gelu(x):
    return torch.nn.functional.gelu(x, approximate="tanh")
def _silu(x):
    return torch.nn.functional.silu(x)
def _sigmoid(x):
    return torch.sigmoid(x)
def _layernorm(x, axis=-1, eps=1e-5):
    m = x.mean(dim=axis, keepdim=True)
    v = x.var(dim=axis, keepdim=True, unbiased=False)
    return (x - m) / torch.sqrt(v + eps)
def _scatter_add(tgt, idx, src, axis=0):
    return tgt.clone().index_add_(axis, idx, src)
def _segment_sum(src, seg, n):
    out = torch.zeros((n,) + src.shape[1:], dtype=src.dtype)
    return out.index_add_(0, seg, src)
def _pad(x, pw):
    flat = tuple(v for pair in reversed(pw) for v in pair)
    return torch.nn.functional.pad(x, flat)

kernel_name = 'parallel_sum__gqa_block_cached__glu_gated_swiglu__decode'

def setup_inputs():

    _w = (lambda a: (lambda t: t.to(torch.bfloat16) if t.is_floating_point() else t)(torch.as_tensor(a)))
    def _A_setup():
        B, Sq, D = 2, 1, 448
        Hq, Hkv, Sk, Dh = 16, 8, 32768, 256
        assert Hq % Hkv == 0, (Hq, Hkv)
        return {
            "x": _w((lambda *s: torch.randn(*s, dtype=torch.bfloat16) * 0.05)(B, Sq, D)),
            "k_cache": _w((lambda *s: torch.randn(*s, dtype=torch.bfloat16) * 3.2)(B, Hkv, Sk, Dh)),
            "v_cache": _w((lambda *s: torch.randn(*s, dtype=torch.bfloat16) * 1.6)(B, Hkv, Sk, Dh)),
            "wq": _w((lambda *s: torch.randn(*s, dtype=torch.bfloat16) * 3.2)(D, Hq * Dh) * (D ** -0.5)),
            "wo": _w((lambda *s: torch.randn(*s, dtype=torch.bfloat16) * 1.6)(Hq * Dh, D) * ((Hq * Dh) ** -0.5)),
        }
    _A_in = _A_setup()

    def _B_setup():
        B, S, D, F = 2, 1, 448, 1792
        return {
            "x": _w((lambda *s: torch.randn(*s, dtype=torch.bfloat16) * 0.05)(B, S, D)),
            "w1": _w((lambda *s: torch.randn(*s, dtype=torch.bfloat16) * 1.6)(D, F) * (2.0 / D) ** 0.5),
            "w3": _w((lambda *s: torch.randn(*s, dtype=torch.bfloat16) * 1.6)(D, F) * (2.0 / D) ** 0.5),
            "w2": _w((lambda *s: torch.randn(*s, dtype=torch.bfloat16) * 1.6)(F, D) * (2.0 / F) ** 0.5),
        }
    _B_in = _B_setup()

    return {
        "A_x": _A_in["x"],
        "A_k_cache": _A_in["k_cache"],
        "A_v_cache": _A_in["v_cache"],
        "A_wq": _A_in["wq"],
        "A_wo": _A_in["wo"],
        "B_x": _B_in["x"],
        "B_w1": _B_in["w1"],
        "B_w3": _B_in["w3"],
        "B_w2": _B_in["w2"],
    }

def reference(A_x, A_k_cache, A_v_cache, A_wq, A_wo, B_x, B_w1, B_w3, B_w2):
    def _A_ref(x, k_cache, v_cache, wq, wo):
        B, Sq, D = x.shape
        Hq, Dh = 16, 256
        Hkv = k_cache.shape[1]
        rep = Hq // Hkv
        q = (lambda x, *d: x.permute(*d))((x @ wq).reshape(B, Sq, Hq, Dh), 0, 2, 1, 3)
        k = (lambda x, n, axis: torch.repeat_interleave(x, n, dim=axis))(k_cache, rep, axis=1)
        v = (lambda x, n, axis: torch.repeat_interleave(x, n, dim=axis))(v_cache, rep, axis=1)
        scores = torch.einsum("bhqd,bhkd->bhqk", q, k) * (Dh ** -0.5)
        p = _softmax(scores, axis=-1)
        o = torch.einsum("bhqk,bhkd->bhqd", p, v)
        return (lambda x, *d: x.permute(*d))(o, 0, 2, 1, 3).reshape(B, Sq, Hq * Dh) @ wo

    def _B_ref(x, w1, w3, w2):
        gate = torch.einsum("bsd,df->bsf", x, w1)
        up = torch.einsum("bsd,df->bsf", x, w3)
        h = _silu(gate) * up
        out = torch.einsum("bsf,fd->bsd", h, w2)
        return out

    x = A_x
    xn = _layernorm(x, axis=-1)
    return x + _A_ref(xn, A_k_cache, A_v_cache, A_wq, A_wo) + _B_ref(xn, B_w1, B_w3, B_w2)
```

```cpp
#include <hip/hip_runtime.h>
#include <hip/hip_bf16.h>
#include <stdint.h>

typedef unsigned v4u __attribute__((ext_vector_type(4)));
typedef unsigned v2u __attribute__((ext_vector_type(2)));
typedef float f32x4 __attribute__((ext_vector_type(4)));
typedef __bf16 bf16x2 __attribute__((ext_vector_type(2)));
typedef __bf16 bf16x8 __attribute__((ext_vector_type(8)));

constexpr int NB = 2, DM = 448, HQ = 16, HKV = 8, SK = 32768, DH = 256, FF = 1792;
constexpr int NQ = HQ * DH;
constexpr int NSPLIT = 16, NWG = NB * HKV * NSPLIT;
constexpr int NW = 4;
constexpr int TK = 4;
constexpr int NE = 6;
constexpr int INIT = 4, INITB = 4, INITC = 4;
constexpr int CAP = 5;
constexpr int TG0 = 300, TGK = 3900, VSPAN = 3440;
constexpr int NT = SK / NSPLIT / 32;
static_assert(NT % 2 == 0 && NE % 2 == 0, "rounds");

constexpr int WS_EPOCH = 0;
constexpr int WS_QG = 4096;
constexpr int WS_ML = 40960;
constexpr int WS_KT = 81920;
constexpr int WS_IN1 = 98304;
constexpr int WS_INF = WS_IN1 + 64 * 16384;
constexpr int WS_IN2 = WS_INF + 56 * 4096;
constexpr int WS_BYTES = WS_IN2 + 56 * 8192;
constexpr int OOB = 0x40000000;
constexpr int SC1 = 16, NTL = 2;
constexpr unsigned SPIN_CAP = 1u << 16;

__device__ __forceinline__ float bflo(unsigned w) { return __builtin_bit_cast(float, w << 16); }
__device__ __forceinline__ float bfhi(unsigned w) { return __builtin_bit_cast(float, w & 0xffff0000u); }
__device__ __forceinline__ unsigned f2bf(float f) { return __builtin_bit_cast(unsigned short, (__bf16)f); }
__device__ __forceinline__ float bfr(float f) { return (float)(__bf16)f; }
__device__ __forceinline__ v4u unit(float a, float b, unsigned tag) { return v4u{__float_as_uint(a), tag, __float_as_uint(b), tag}; }
__device__ __forceinline__ bool unit_ok(v4u u, unsigned tag) { return (u.y == tag) & (u.w == tag); }
__device__ __forceinline__ float dot2(unsigned a, unsigned b, float c) {
  return __builtin_amdgcn_fdot2_f32_bf16(__builtin_bit_cast(bf16x2, a), __builtin_bit_cast(bf16x2, b), c, false);
}
template <int CTRL> __device__ __forceinline__ float dpp(float x) {
  return __builtin_bit_cast(float, __builtin_amdgcn_mov_dpp(__builtin_bit_cast(int, x), CTRL, 0xf, 0xf, true));
}
__device__ __forceinline__ float rdlane(float x, int l) { return __builtin_bit_cast(float, __builtin_amdgcn_readlane(__builtin_bit_cast(int, x), l)); }
constexpr int XOR1 = 0xB1, XOR2 = 0x4E, XOR7 = 0x141, XOR8 = 0x128, SHL4 = 0x104;
__device__ __forceinline__ float swap16_add(float a, float b) {
  auto r = __builtin_amdgcn_permlane16_swap(__float_as_uint(a), __float_as_uint(b), false, false);
  return __uint_as_float(r[0]) + __uint_as_float(r[1]);
}
__device__ __forceinline__ float swap32_add(float a, float b) {
  auto r = __builtin_amdgcn_permlane32_swap(__float_as_uint(a), __float_as_uint(b), false, false);
  return __uint_as_float(r[0]) + __uint_as_float(r[1]);
}
__device__ __forceinline__ float swap16_max(float a) {
  auto r = __builtin_amdgcn_permlane16_swap(__float_as_uint(a), __float_as_uint(a), false, false);
  return fmaxf(__uint_as_float(r[0]), __uint_as_float(r[1]));
}
__device__ __forceinline__ float swap32_max(float a) {
  auto r = __builtin_amdgcn_permlane32_swap(__float_as_uint(a), __float_as_uint(a), false, false);
  return fmaxf(__uint_as_float(r[0]), __uint_as_float(r[1]));
}
__device__ __forceinline__ float wave_allsum(float v) {
  v += dpp<XOR1>(v); v += dpp<XOR2>(v); v += dpp<XOR7>(v); v += dpp<XOR8>(v);
  v = swap16_add(v, v); v = swap32_add(v, v);
  return v;
}
__device__ __forceinline__ float wave_allmax(float v) {
  v = fmaxf(v, dpp<XOR1>(v)); v = fmaxf(v, dpp<XOR2>(v)); v = fmaxf(v, dpp<XOR7>(v)); v = fmaxf(v, dpp<XOR8>(v));
  return swap32_max(swap16_max(v));
}
__device__ __forceinline__ float sum_b345(float v) {
  v += dpp<XOR8>(v); v = swap16_add(v, v); v = swap32_add(v, v);
  return v;
}
__device__ __forceinline__ float reduce16(float (&a)[16], int lane) {
  const bool b3 = lane & 8, b1 = lane & 2;
#pragma unroll
  for (int i = 0; i < 8; ++i) a[i] = swap32_add(a[i], a[i + 8]);
#pragma unroll
  for (int i = 0; i < 4; ++i) a[i] = swap16_add(a[i], a[i + 4]);
#pragma unroll
  for (int i = 0; i < 2; ++i) { const float keep = b3 ? a[i + 2] : a[i], send = b3 ? a[i] : a[i + 2]; a[i] = keep + dpp<XOR8>(send); }
  const float keep = b1 ? a[1] : a[0], send = b1 ? a[0] : a[1];
  const float v = keep + dpp<XOR2>(send);
  return v + dpp<SHL4>(v);
}
__device__ __forceinline__ void gemv_row(float (&a)[16], v4u wv, float x0, float x1) {
#pragma unroll
  for (int k = 0; k < 4; ++k) {
    a[2 * k] = fmaf(x0, bflo(wv[k]), a[2 * k]); a[2 * k + 1] = fmaf(x0, bfhi(wv[k]), a[2 * k + 1]);
    a[8 + 2 * k] = fmaf(x1, bflo(wv[k]), a[8 + 2 * k]); a[8 + 2 * k + 1] = fmaf(x1, bfhi(wv[k]), a[8 + 2 * k + 1]);
  }
}
#define RSRC(p, n) __builtin_amdgcn_make_buffer_rsrc((void*)(p), 0, (n), 0x00020000)
#define LD128(rs, off, aux) __builtin_amdgcn_raw_buffer_load_b128((rs), (off), 0, (aux))
#define LD32(rs, off, aux) __builtin_amdgcn_raw_buffer_load_b32((rs), (off), 0, (aux))
#define ST128(v, rs, off, aux) __builtin_amdgcn_raw_buffer_store_b128((v), (rs), (off), 0, (aux))
#define ST64(v, rs, off, aux) __builtin_amdgcn_raw_buffer_store_b64((v), (rs), (off), 0, (aux))
#define ST32(v, rs, off, aux) __builtin_amdgcn_raw_buffer_store_b32((v), (rs), (off), 0, (aux))
#define LDSV(x) (*(volatile __attribute__((address_space(3))) int*)&(x))
#define MEMFENCE() asm volatile("" ::: "memory")
#define SCHED() __builtin_amdgcn_sched_barrier(0)
#define PIN8(a, o) asm volatile("" : "+v"(a[o]), "+v"(a[o + 1]), "+v"(a[o + 2]), "+v"(a[o + 3]), "+v"(a[o + 4]), "+v"(a[o + 5]), "+v"(a[o + 6]), "+v"(a[o + 7]) :: "memory")
#define VMWAIT(n) asm volatile("s_waitcnt vmcnt(%0)" :: "i"(n) : "memory")

struct __attribute__((aligned(16))) Lds {
  union {
    unsigned wot[32][DM];
    struct { float xn[2][512]; float fo[896]; } a;
  } u;
  float sc[NW][NT * 16];
  float red[NW][32];
  unsigned qs[256];
  unsigned short ov[2][64];
  unsigned tag;
  int anchor, kdone, pad;
};

extern "C" __global__ void __launch_bounds__(NW * 64 + 64, 2)
fused_decode(const unsigned short* __restrict__ x, const unsigned short* __restrict__ kc, const unsigned short* __restrict__ vc,
             const unsigned short* __restrict__ wq, const unsigned short* __restrict__ wo, const unsigned short* __restrict__ w1,
             const unsigned short* __restrict__ w3, const unsigned short* __restrict__ w2, unsigned short* out, unsigned char* ws) {
  __shared__ Lds L;
  asm volatile("" :: "s"(x), "s"(kc), "s"(vc), "s"(wq), "s"(wo), "s"(w1), "s"(w3), "s"(w2), "s"(out), "s"(ws));
  const int tid = threadIdx.x, lane = tid & 63, w = __builtin_amdgcn_readfirstlane(tid >> 6);
  const int j = blockIdx.x, hkv = j & 7, r = j >> 3;
  const int b = j >> 7, ha = (j >> 4) & 7, sp = j & 15;
  const int g = j >> 4;
  const bool heavy = sp & 1;
  const bool own_rows = sp < 8 && heavy;
  const bool own_out = sp < 8 && !heavy && g < 14;
  const bool own_ffn = sp >= 8 && heavy && g < 14 && ((sp >> 1) & 1) == (g & 1);
  const int ro = g * 4 + ((sp >> 1) & 3), jf = g * 2 + (sp >= 12 ? 1 : 0);
  const int c = lane & 31, hf = lane & 1;
  const bool b3 = lane & 8, b2 = lane & 4, b1 = lane & 2;
  constexpr float C2 = 0.0625f * 1.4426950408889634f;
  const __amdgpu_buffer_rsrc_t rws = RSRC(ws, WS_BYTES);
  const __amdgpu_buffer_rsrc_t rx = RSRC(x, NB * DM * 2);

  constexpr int TILE_B = TK * 1024, STEP_B = NW * TILE_B;
  constexpr int CHUNK_B = 2 * NT * STEP_B, ROUND_B = 2 * STEP_B;
  const size_t base = (size_t)g * SK * DH + (size_t)(sp & 7) * (CHUNK_B / 2);
  const __amdgpu_buffer_rsrc_t rk = RSRC(kc + base, CHUNK_B);
  const __amdgpu_buffer_rsrc_t rv = RSRC(vc + base, CHUNK_B);

  if (w < NW) {
    const unsigned long long t_begin = __builtin_amdgcn_s_memrealtime();
    constexpr int RPW = DM / NW, NRL = (RPW + 31) / 32;
    const __amdgpu_buffer_rsrc_t rwq = RSRC(wq, DM * NQ * 2);
    const int rl = lane >> 1, row0 = RPW * w + rl;
    const v4u xs0 = LD128(rx, lane < 56 ? lane * 16 : OOB, 0);
    const v4u xs1 = LD128(rx, lane < 56 ? DM * 2 + lane * 16 : OOB, 0);
    SCHED();
    v4u wqv[NRL];
#pragma unroll
    for (int i = 0; i < NRL; ++i) wqv[i] = LD128(rwq, rl + 32 * i < RPW ? (row0 + 32 * i) * (NQ * 2) + (512 * hkv + 16 * r + 8 * hf) * 2 : OOB, NTL);
    SCHED();
    const int vo0 = (sp >> 3) * STEP_B + w * TILE_B + lane * 16;
    v4u T[NE + 2][TK];
#define KREQ(i) T[(i) / TK][(i) % TK] = LD128(rk, vo0 + ((i) / TK) * ROUND_B + ((i) % TK) * 1024, NTL)
#pragma unroll
    for (int i = 0; i < INIT; ++i) KREQ(i);
    SCHED();
    {
      float s0 = 0.f, s1 = 0.f;
#pragma unroll
      for (int k = 0; k < 4; ++k) { s0 += bflo(xs0[k]) + bfhi(xs0[k]); s1 += bflo(xs1[k]) + bfhi(xs1[k]); }
      s0 = wave_allsum(s0); s1 = wave_allsum(s1);
      const float m0 = s0 * (1.0f / DM), m1 = s1 * (1.0f / DM);
      float q0 = 0.f, q1 = 0.f;
#pragma unroll
      for (int k = 0; k < 4; ++k) {
        float d = bflo(xs0[k]) - m0; q0 = fmaf(d, d, q0); d = bfhi(xs0[k]) - m0; q0 = fmaf(d, d, q0);
        d = bflo(xs1[k]) - m1; q1 = fmaf(d, d, q1); d = bfhi(xs1[k]) - m1; q1 = fmaf(d, d, q1);
      }
      if (lane >= 56) { q0 = 0.f; q1 = 0.f; }
      q0 = wave_allsum(q0); q1 = wave_allsum(q1);
      const float mb0 = bfr(m0), mb1 = bfr(m1);
      const float is0 = lane < 56 ? __builtin_amdgcn_rcpf(bfr(__builtin_amdgcn_sqrtf(bfr(bfr(q0 * (1.0f / DM)) + 1e-5f)))) : 0.f;
      const float is1 = lane < 56 ? __builtin_amdgcn_rcpf(bfr(__builtin_amdgcn_sqrtf(bfr(bfr(q1 * (1.0f / DM)) + 1e-5f)))) : 0.f;
      float n0[8], n1[8];
#pragma unroll
      for (int k = 0; k < 4; ++k) {
        n0[2 * k] = bfr(bfr(bflo(xs0[k]) - mb0) * is0); n0[2 * k + 1] = bfr(bfr(bfhi(xs0[k]) - mb0) * is0);
        n1[2 * k] = bfr(bfr(bflo(xs1[k]) - mb1) * is1); n1[2 * k + 1] = bfr(bfr(bfhi(xs1[k]) - mb1) * is1);
      }
      *(f32x4*)&L.u.a.xn[0][lane * 8] = f32x4{n0[0], n0[1], n0[2], n0[3]}; *(f32x4*)&L.u.a.xn[0][lane * 8 + 4] = f32x4{n0[4], n0[5], n0[6], n0[7]};
      *(f32x4*)&L.u.a.xn[1][lane * 8] = f32x4{n1[0], n1[1], n1[2], n1[3]}; *(f32x4*)&L.u.a.xn[1][lane * 8 + 4] = f32x4{n1[4], n1[5], n1[6], n1[7]};
    }
    MEMFENCE(); SCHED();
#pragma unroll
    for (int i = INIT; i < INIT + INITB; ++i) KREQ(i);
    SCHED();
    {
      float a[16];
#pragma unroll
      for (int k = 0; k < 16; ++k) a[k] = 0.f;
#pragma unroll
      for (int i = 0; i < NRL; ++i) gemv_row(a, wqv[i], L.u.a.xn[0][row0 + 32 * i], L.u.a.xn[1][row0 + 32 * i]);
      PIN8(a, 0); PIN8(a, 8); SCHED();
#pragma unroll
      for (int i = INIT + INITB; i < INIT + INITB + INITC; ++i) KREQ(i);
      SCHED();
      const float v = reduce16(a, lane);
      if (!b2) L.red[w][(lane >> 5) * 16 + hf * 8 + ((lane >> 4) & 1) * 4 + ((lane >> 3) & 1) * 2 + ((lane >> 1) & 1)] = v;
    }
    __syncthreads();
#pragma unroll
    for (int i = INIT + INITB + INITC; i < (NE + 2) * TK; ++i) { VMWAIT(CAP - 1); KREQ(i); SCHED(); }
    __syncthreads();
    const v4u q0 = *(const v4u*)&L.qs[c * 4], q1 = *(const v4u*)&L.qs[128 + c * 4];
    const unsigned tag = L.tag;

    float mx = -3.0e38f;
    float* sw = &L.sc[w][lane >> 2];
    bool hold = true;
    constexpr unsigned kstep = (unsigned)((TGK - TG0) << 8) / NT, vstep = (unsigned)(VSPAN << 8) / NT;
    auto pace = [&](unsigned due) __attribute__((always_inline)) { hold = (int)((unsigned)__builtin_amdgcn_s_memrealtime() - due) < 0; };
#define HOLD() do { if (hold) VMWAIT(CAP - 1); } while (0)
    auto ktile = [&](v4u (&K)[TK], int slot, int buf) __attribute__((always_inline)) {
      float xx[2 * TK];
      const bool tov = slot + 2 >= NT;
      const int vn = vo0 + (tov ? buf : slot + 2) * ROUND_B;
      const __amdgpu_buffer_rsrc_t rs = RSRC((tov ? vc : kc) + base, CHUNK_B);
#pragma unroll
      for (int t = 0; t < TK; ++t) {
        float d0 = 0.f, d1 = 0.f;
#pragma unroll
        for (int k = 0; k < 4; ++k) { d0 = dot2(K[t][k], q0[k], d0); d1 = dot2(K[t][k], q1[k], d1); }
        xx[2 * t] = d0; xx[2 * t + 1] = d1;
        if (buf >= 0) { asm volatile("" : "+v"(xx[2 * t]), "+v"(xx[2 * t + 1]) :: "memory"); SCHED(); HOLD(); K[t] = LD128(rs, vn + t * 1024, NTL); SCHED(); }
      }
#pragma unroll
      for (int i = 0; i < 4; ++i) xx[i] = swap16_add(xx[i], xx[i + 4]);
#pragma unroll
      for (int i = 0; i < 2; ++i) { const float keep = b3 ? xx[i + 2] : xx[i], send = b3 ? xx[i] : xx[i + 2]; xx[i] = keep + dpp<XOR8>(send); }
      const float keep = b2 ? xx[1] : xx[0], send = b2 ? xx[0] : xx[1];
      float s = keep + dpp<XOR7>(send);
      s += dpp<XOR2>(s);
      s = bfr(s + dpp<XOR1>(s));
      mx = fmaxf(mx, s);
      sw[slot * 16] = s;
      SCHED();
    };
    constexpr int NR0 = (NE / 2 + 1) & ~1;
#pragma unroll
    for (int i = 0; i < NR0; ++i) {
      ktile(T[NE + (i & 1)], NE + i, i & 1);
      if (2 * i < NE) { ktile(T[2 * i], 2 * i, -1); ktile(T[2 * i + 1], 2 * i + 1, -1); }
    }
    for (int s = NE + NR0; s < NT; s += 2) {
      pace((unsigned)t_begin + TG0 + (((unsigned)(s + 2) * kstep) >> 8));
      if (s + 2 >= NT) hold = false;
      ktile(T[NE], s, 0); ktile(T[NE + 1], s + 1, 1);
    }
    const unsigned tk = (unsigned)__builtin_amdgcn_s_memrealtime();
    if (tid == 0) LDSV(L.kdone) = 1;
    float m0, m1, l0 = 0.f, l1 = 0.f;
    {
      mx = fmaxf(mx, dpp<XOR8>(mx)); mx = swap16_max(mx); mx = swap32_max(mx);
      m0 = rdlane(mx, 0); m1 = rdlane(mx, 4);
      f32x4* pp = (f32x4*)&L.sc[w][lane * 4];
#pragma unroll
      for (int i = 0; i < NT * TK / 64; ++i) {
        f32x4 v = pp[i * 64];
        v.x = __builtin_amdgcn_exp2f((v.x - m0) * C2); v.y = __builtin_amdgcn_exp2f((v.y - m1) * C2);
        v.z = __builtin_amdgcn_exp2f((v.z - m0) * C2); v.w = __builtin_amdgcn_exp2f((v.w - m1) * C2);
        l0 += v.x + v.z; l1 += v.y + v.w;
        pp[i * 64] = v;
      }
      l0 = wave_allsum(l0); l1 = wave_allsum(l1);
      ST128(unit(lane == 2 ? __uint_as_float(tk) : (lane ? m1 : m0), lane ? l1 : l0, tag), rws,
            lane < 2 ? WS_ML + g * 2048 + lane * 1024 + (sp * 4 + w) * 16 : (lane == 2 ? WS_KT + g * 1024 + (sp * 4 + w) * 16 : OOB), SC1);
    }
    float acc0[8], acc1[8];
#pragma unroll
    for (int k = 0; k < 8; ++k) { acc0[k] = 0.f; acc1[k] = 0.f; }
    const f32x4* pr = (const f32x4*)&L.sc[w][(lane >> 5) * (2 * TK)];
    auto vtile = [&](v4u (&V)[TK], int slot) __attribute__((always_inline)) {
      const int vn = vo0 + (slot + 2) * ROUND_B;
      f32x4 p[TK / 2];
#pragma unroll
      for (int i = 0; i < TK / 2; ++i) p[i] = pr[slot * TK + i];
#pragma unroll
      for (int t = 0; t < TK; ++t) {
        const float p0 = p[t >> 1][(t & 1) * 2], p1 = p[t >> 1][(t & 1) * 2 + 1];
#pragma unroll
        for (int k = 0; k < 4; ++k) {
          const float lo = bflo(V[t][k]), hi = bfhi(V[t][k]);
          acc0[2 * k] = fmaf(p0, lo, acc0[2 * k]); acc0[2 * k + 1] = fmaf(p0, hi, acc0[2 * k + 1]);
          acc1[2 * k] = fmaf(p1, lo, acc1[2 * k]); acc1[2 * k + 1] = fmaf(p1, hi, acc1[2 * k + 1]);
        }
        PIN8(acc0, 0); PIN8(acc1, 0); SCHED(); HOLD(); V[t] = LD128(rv, vn + t * 1024, NTL); SCHED();
      }
    };
    constexpr int GAP = 4;
    constexpr int s1 = NT - 2 * GAP;
    float f0 = 0.f, f1 = 0.f;
    v4u ma = {0u, 0u, 0u, 0u}, mb = ma;
    for (int s = 0; s < s1; s += 2) {
      pace((unsigned)__builtin_amdgcn_readfirstlane(LDSV(L.anchor)) + (((unsigned)(s + 2) * vstep) >> 8));
      vtile(T[NE], s); vtile(T[NE + 1], s + 1);
    }
    hold = false;
    ma = LD128(rws, WS_ML + g * 2048 + lane * 16, SC1); mb = LD128(rws, WS_ML + g * 2048 + 1024 + lane * 16, SC1);
    SCHED();
#pragma unroll
    for (int i = 0; i < GAP; ++i) vtile(T[NE + i % 2], s1 + i);
    if (!__all(unit_ok(ma, tag) & unit_ok(mb, tag))) {
      for (unsigned spins = 0; spins < SPIN_CAP; ++spins) {
        MEMFENCE();
        ma = LD128(rws, WS_ML + g * 2048 + lane * 16, SC1); mb = LD128(rws, WS_ML + g * 2048 + 1024 + lane * 16, SC1);
        if (__all(unit_ok(ma, tag) & unit_ok(mb, tag))) break;
      }
    }
    {
      const float M0 = wave_allmax(__uint_as_float(ma.x)), M1 = wave_allmax(__uint_as_float(mb.x));
      const float L0 = wave_allsum(__uint_as_float(ma.z) * __builtin_amdgcn_exp2f((__uint_as_float(ma.x) - M0) * C2));
      const float L1 = wave_allsum(__uint_as_float(mb.z) * __builtin_amdgcn_exp2f((__uint_as_float(mb.x) - M1) * C2));
      f0 = __builtin_amdgcn_exp2f((m0 - M0) * C2) / L0; f1 = __builtin_amdgcn_exp2f((m1 - M1) * C2) / L1;
      SCHED();
    }
#pragma unroll 1
    for (int s = s1 + GAP; s < NT; s += 2) { vtile(T[NE], s); vtile(T[NE + 1], s + 1); }
    __builtin_amdgcn_s_waitcnt(0x0F70);
    {
#pragma unroll
      for (int k = 0; k < 8; ++k) acc0[k] = swap32_add(acc0[k] * f0, acc1[k] * f1);
      float* mg = &L.sc[w][(lane >> 5) * 256 + c * 8];
      *(f32x4*)mg = f32x4{acc0[0], acc0[1], acc0[2], acc0[3]};
      *(f32x4*)(mg + 4) = f32x4{acc0[4], acc0[5], acc0[6], acc0[7]};
    }
    __syncthreads();
    {
      float o0 = 0.f, o1 = 0.f;
#pragma unroll
      for (int ww = 0; ww < NW; ++ww) { const float2 t = *(const float2*)&L.sc[ww][w * 128 + lane * 2]; o0 += t.x; o1 += t.y; }
      ST128(unit(o0, o1, tag), rws, WS_IN1 + (ha * 8 + w * 2 + (lane >> 5)) * 16384 + (b * 16 + sp) * 512 + c * 16, SC1);
    }
    if (own_rows) {
      v4u wb[7][2];
#pragma unroll
      for (int t = 0; t < 7; ++t)
#pragma unroll
        for (int ks = 0; ks < 2; ++ks)
#pragma unroll
          for (int e = 0; e < 4; ++e) wb[t][ks][e] = L.u.wot[ks * 16 + (lane >> 4) * 4 + e][w * 112 + t * 16 + (lane & 15)];
      {
        const int src = WS_IN1 + ro * 16384 + (lane >> 3) * 512 + w * 128 + (lane & 7) * 16;
        float va[4], vb[4]; bool ok[4];
#pragma unroll
        for (int i = 0; i < 4; ++i) { va[i] = 0.f; vb[i] = 0.f; ok[i] = false; }
        for (unsigned spins = 0; spins < SPIN_CAP; ++spins) {
          MEMFENCE();
          v4u n[4]; bool all = true;
#pragma unroll
          for (int i = 0; i < 4; ++i) n[i] = LD128(rws, ok[i] ? OOB : src + i * 4096, SC1);
#pragma unroll
          for (int i = 0; i < 4; ++i) {
            va[i] = ok[i] ? va[i] : __uint_as_float(n[i].x); vb[i] = ok[i] ? vb[i] : __uint_as_float(n[i].z);
            ok[i] = ok[i] | unit_ok(n[i], tag); all = all & ok[i];
          }
          if (__all(all)) break;
        }
        float oa = swap32_add(va[0] + va[1], va[2] + va[3]), ob = swap32_add(vb[0] + vb[1], vb[2] + vb[3]);
        oa = swap16_add(oa, oa); ob = swap16_add(ob, ob);
        oa += dpp<XOR8>(oa); ob += dpp<XOR8>(ob);
        if (!(lane & 24)) *(unsigned*)&L.ov[lane >> 5][w * 16 + (lane & 7) * 2] = f2bf(oa) | (f2bf(ob) << 16);
      }
      __syncthreads();
      {
        v4u oa[2];
#pragma unroll
        for (int ks = 0; ks < 2; ++ks) {
          const v4u t = *(const v4u*)&L.ov[lane & 1][ks * 32 + (lane >> 4) * 8];
          oa[ks] = (lane & 14) ? v4u{0u, 0u, 0u, 0u} : t;
        }
        f32x4 d[7];
#pragma unroll
        for (int t = 0; t < 7; ++t) d[t] = __builtin_amdgcn_mfma_f32_16x16x32_bf16(__builtin_bit_cast(bf16x8, oa[0]), __builtin_bit_cast(bf16x8, wb[t][0]), f32x4{0.f, 0.f, 0.f, 0.f}, 0, 0, 0);
#pragma unroll
        for (int t = 0; t < 7; ++t) d[t] = __builtin_amdgcn_mfma_f32_16x16x32_bf16(__builtin_bit_cast(bf16x8, oa[1]), __builtin_bit_cast(bf16x8, wb[t][1]), d[t], 0, 0, 0);
        const int dst = lane < 16 ? WS_IN2 + (hf * 28 + w * 7) * 8192 + ro * 128 + (lane >> 1) * 16 : OOB;
#pragma unroll
        for (int t = 0; t < 7; ++t) {
          const float other = dpp<XOR1>(hf ? d[t].x : d[t].y);
          ST128(unit(hf ? other : d[t].x, hf ? d[t].y : other, tag), rws, dst + t * 8192, SC1);
        }
      }
    }
    if (own_out) {
      float va[2] = {0.f, 0.f}, vb[2] = {0.f, 0.f}; bool ok[2] = {false, false};
      for (unsigned spins = 0; spins < SPIN_CAP; ++spins) {
        MEMFENCE();
        v4u n[2];
#pragma unroll
        for (int i = 0; i < 2; ++i) n[i] = LD128(rws, ok[i] ? OOB : WS_IN2 + ro * 8192 + (w * 2 + i) * 1024 + lane * 16, SC1);
#pragma unroll
        for (int i = 0; i < 2; ++i) {
          va[i] = ok[i] ? va[i] : __uint_as_float(n[i].x); vb[i] = ok[i] ? vb[i] : __uint_as_float(n[i].z);
          ok[i] = ok[i] | unit_ok(n[i], tag);
        }
        if (__all(ok[0] & ok[1])) break;
      }
      const float a0 = sum_b345(va[0] + va[1]), a1 = sum_b345(vb[0] + vb[1]);
      if (lane < 8) *(float2*)&L.red[w][lane * 2] = float2{a0, a1};
      __syncthreads();
    }
  } else {
    const unsigned h_begin = (unsigned)__builtin_amdgcn_s_memrealtime();
    const unsigned epoch = LD32(rws, WS_EPOCH, SC1);
    SCHED();
    const unsigned warm = LD32(rv, lane == 0 ? 0 : (lane == 1 ? CHUNK_B - 4 : OOB), NTL) ^ LD32(rk, lane == 0 ? CHUNK_B - 4 : OOB, NTL);
    SCHED();
    __syncthreads();
    const unsigned tag = epoch + 1u;
    {
      const int bb = (lane >> 3) & 1, pr = lane & 7;
      float s0 = 0.f, s1 = 0.f;
#pragma unroll
      for (int ww = 0; ww < NW; ++ww) { s0 += L.red[ww][bb * 16 + 2 * pr]; s1 += L.red[ww][bb * 16 + 2 * pr + 1]; }
      const v2u gr = {f2bf(s0) | (f2bf(s1) << 16), tag};
      ST64(gr, rws, lane < 16 ? WS_QG + ((bb * 8 + hkv) * 256 + 8 * r + pr) * 8 : OOB, SC1);
    }
    {
      const int qo = WS_QG + g * 2048 + lane * 16;
      __builtin_amdgcn_s_sleep(50);
      for (unsigned spins = 0; spins < SPIN_CAP; ++spins) {
        MEMFENCE();
        const v4u g0 = LD128(rws, qo, SC1), g1 = LD128(rws, qo + 1024, SC1);
        *(v2u*)&L.qs[lane * 2] = v2u{g0.x, g0.z}; *(v2u*)&L.qs[128 + lane * 2] = v2u{g1.x, g1.z};
        if (__all(unit_ok(g0, tag) & unit_ok(g1, tag))) break;
      }
      if (lane == 0) { L.tag = tag; L.anchor = (int)(h_begin + TGK); L.kdone = 0; }
    }
    __syncthreads();
    asm volatile("" :: "v"(warm));
    if (own_rows) {
      const __amdgpu_buffer_rsrc_t rwo = RSRC(wo + (size_t)(64 * ro) * DM, 64 * DM * 2);
#pragma unroll 1
      for (int i = 0; i < 4; ++i) {
        v4u t0[8], t1[8];
#pragma unroll
        for (int k = 0; k < 8; ++k) {
          const int off = lane < 56 ? (i * 8 + k) * (4 * DM) + lane * 16 : OOB;
          t0[k] = LD128(rwo, off, NTL); t1[k] = LD128(rwo, off + 2 * DM, NTL);
        }
        if (lane < 56) {
#pragma unroll
          for (int k = 0; k < 8; ++k) {
            v4u lo, hi;
#pragma unroll
            for (int e = 0; e < 2; ++e) {
              lo[2 * e] = __builtin_amdgcn_perm(t1[k][e], t0[k][e], 0x05040100u); lo[2 * e + 1] = __builtin_amdgcn_perm(t1[k][e], t0[k][e], 0x07060302u);
              hi[2 * e] = __builtin_amdgcn_perm(t1[k][2 + e], t0[k][2 + e], 0x05040100u); hi[2 * e + 1] = __builtin_amdgcn_perm(t1[k][2 + e], t0[k][2 + e], 0x07060302u);
            }
            *(v4u*)&L.u.wot[i * 8 + k][lane * 8] = lo; *(v4u*)&L.u.wot[i * 8 + k][lane * 8 + 4] = hi;
          }
        }
      }
    } else if (own_ffn) {
      const __amdgpu_buffer_rsrc_t rw1 = RSRC(w1, DM * FF * 2), rw3 = RSRC(w3, DM * FF * 2);
      const int rg = lane >> 3, wo0 = rg * (FF * 2) + jf * 128 + (lane & 7) * 16;
      float gt[16], u[16];
#pragma unroll
      for (int k = 0; k < 16; ++k) { gt[k] = 0.f; u[k] = 0.f; }
      const __amdgpu_buffer_rsrc_t rw2 = RSRC(w2 + (size_t)jf * 64 * DM, 64 * DM * 2);
      v4u ta[16], tb[16];
      auto ld13 = [&](v4u (&t)[16], int i) __attribute__((always_inline)) {
#pragma unroll
        for (int k = 0; k < 7; ++k) { t[2 * k] = LD128(rw1, wo0 + (i * 56 + k * 8) * (FF * 2), NTL); t[2 * k + 1] = LD128(rw3, wo0 + (i * 56 + k * 8) * (FF * 2), NTL); }
        SCHED();
      };
      auto ld2 = [&](v4u (&t)[16], int i) __attribute__((always_inline)) {
#pragma unroll
        for (int k = 0; k < 16; ++k) t[k] = LD128(rw2, lane < 56 ? (i * 16 + k) * (DM * 2) + lane * 16 : OOB, NTL);
        SCHED();
      };
      auto use13 = [&](v4u (&t)[16], int i) __attribute__((always_inline)) {
#pragma unroll
        for (int k = 0; k < 7; ++k) { const float x0 = L.u.a.xn[0][i * 56 + k * 8 + rg], x1 = L.u.a.xn[1][i * 56 + k * 8 + rg]; gemv_row(gt, t[2 * k], x0, x1); gemv_row(u, t[2 * k + 1], x0, x1); }
        PIN8(gt, 0); PIN8(gt, 8); PIN8(u, 0); PIN8(u, 8);
        SCHED();
      };
      ld13(ta, 0); ld13(tb, 1);
#pragma unroll 1
      for (int i = 0; i < 6; i += 2) { use13(ta, i); ld13(ta, i + 2); use13(tb, i + 1); ld13(tb, i + 3); }
      use13(ta, 6); ld2(ta, 0); use13(tb, 7); ld2(tb, 1);
#pragma unroll
      for (int bb = 0; bb < 16; bb += 8) {
#pragma unroll
        for (int k = 0; k < 4; ++k) { gt[bb + k] = swap32_add(gt[bb + k], gt[bb + k + 4]); u[bb + k] = swap32_add(u[bb + k], u[bb + k + 4]); }
#pragma unroll
        for (int k = 0; k < 2; ++k) { gt[bb + k] = swap16_add(gt[bb + k], gt[bb + k + 2]); u[bb + k] = swap16_add(u[bb + k], u[bb + k + 2]); }
        { const float keep = b3 ? gt[bb + 1] : gt[bb], send = b3 ? gt[bb] : gt[bb + 1]; gt[bb] = bfr(keep + dpp<XOR8>(send)); }
        { const float keep = b3 ? u[bb + 1] : u[bb], send = b3 ? u[bb] : u[bb + 1]; u[bb] = bfr(keep + dpp<XOR8>(send)); }
      }
      const float h0 = bfr(bfr(gt[0] / (1.0f + __expf(-gt[0]))) * u[0]), h1 = bfr(bfr(gt[8] / (1.0f + __expf(-gt[8]))) * u[8]);
      float f[16];
#pragma unroll
      for (int k = 0; k < 16; ++k) f[k] = 0.f;
      auto use2 = [&](v4u (&t)[16], int i) __attribute__((always_inline)) {
#pragma unroll
        for (int k = 0; k < 16; ++k) {
          const int col = i * 16 + k, src = (col >> 3) + ((col >> 2) & 1) * 32 + ((col >> 1) & 1) * 16 + (col & 1) * 8;
          gemv_row(f, t[k], rdlane(h0, src), rdlane(h1, src));
        }
        PIN8(f, 0); PIN8(f, 8);
        SCHED();
      };
      use2(ta, 0); ld2(ta, 2); use2(tb, 1); ld2(tb, 3); use2(ta, 2); use2(tb, 3);
      if (lane < 56) {
        *(f32x4*)&L.u.a.fo[lane * 8] = f32x4{f[0], f[1], f[2], f[3]}; *(f32x4*)&L.u.a.fo[lane * 8 + 4] = f32x4{f[4], f[5], f[6], f[7]};
        *(f32x4*)&L.u.a.fo[DM + lane * 8] = f32x4{f[8], f[9], f[10], f[11]}; *(f32x4*)&L.u.a.fo[DM + lane * 8 + 4] = f32x4{f[12], f[13], f[14], f[15]};
      }
#pragma unroll
      for (int i = 0; i < 7; ++i) {
        const float2 t = *(const float2*)&L.u.a.fo[(i * 64 + lane) * 2];
        ST128(unit(t.x, t.y, tag), rws, WS_INF + (i * 8 + (lane >> 3)) * 4096 + jf * 128 + (lane & 7) * 16, SC1);
      }
    }
    {
      for (unsigned spins = 0; spins < 4096 && LDSV(L.kdone) == 0; ++spins) __builtin_amdgcn_s_sleep(16);
      __builtin_amdgcn_s_sleep(127);
      const v4u t = LD128(rws, WS_KT + g * 1024 + lane * 16, SC1);
      const bool ok = unit_ok(t, tag);
      const float sum = wave_allsum(ok ? (float)(int)(t.x - h_begin) : 0.f), cnt = wave_allsum(ok ? 1.f : 0.f);
      if (lane == 0 && cnt >= 32.f) LDSV(L.anchor) = (int)(h_begin + (unsigned)(int)(sum / cnt));
    }
    if (own_rows) __syncthreads();
    if (own_out) {
      const int jo = ro;
      float f0 = 0.f, f1 = 0.f;
      const unsigned xv = LD32(rx, (jo * 16 + (lane & 7) * 2) * 2, 0);
      {
        v4u t[4];
        for (unsigned spins = 0;; ++spins) {
          MEMFENCE();
          bool all = true;
#pragma unroll
          for (int i = 0; i < 4; ++i) t[i] = LD128(rws, i * 64 + lane < 224 ? WS_INF + jo * 4096 + i * 1024 + lane * 16 : OOB, SC1);
#pragma unroll
          for (int i = 0; i < 4; ++i) all = all && (i * 64 + lane >= 224 || unit_ok(t[i], tag));
          if (__all(all) || spins > SPIN_CAP) break;
          __builtin_amdgcn_s_sleep(64);
        }
#pragma unroll
        for (int i = 0; i < 4; ++i) { f0 += __uint_as_float(t[i].x); f1 += __uint_as_float(t[i].z); }
        f0 = sum_b345(f0); f1 = sum_b345(f1);
      }
      __syncthreads();
      __syncthreads();
      float a0 = 0.f, a1 = 0.f;
#pragma unroll
      for (int ww = 0; ww < NW; ++ww) { const float2 t = *(const float2*)&L.red[ww][(lane & 7) * 2]; a0 += t.x; a1 += t.y; }
      if (lane < 8) {
        const unsigned o0 = f2bf(bfr(bflo(xv) + bfr(a0)) + bfr(f0)), o1 = f2bf(bfr(bfhi(xv) + bfr(a1)) + bfr(f1));
        *(unsigned*)(out + jo * 16 + lane * 2) = o0 | (o1 << 16);
        if (jo == 0 && lane == 0) ST32(tag, rws, WS_EPOCH, SC1);
      }
    }
  }
}

extern "C" void kernel_launch(void* const* d_in, const int* in_sizes, int n_in, void* d_out, int out_size, void* d_ws, size_t ws_size, hipStream_t stream) {
  fused_decode<<<NWG, NW * 64 + 64, 0, stream>>>((const unsigned short*)d_in[0], (const unsigned short*)d_in[1], (const unsigned short*)d_in[2], (const unsigned short*)d_in[3],
                                                 (const unsigned short*)d_in[4], (const unsigned short*)d_in[6], (const unsigned short*)d_in[7], (const unsigned short*)d_in[8],
                                                 (unsigned short*)d_out, (unsigned char*)d_ws);
}
```
